```python
import torch

def _softmax(x, axis=-1):
    return torch.softmax(x, dim=axis)
def _gelu(x):
    return torch.nn.functional.gelu(x, approximate="tanh")
def _silu(x):
    return torch.nn.functional.silu(x)
def _sigmoid(x):
    return torch.sigmoid(x)
def _layernorm(x, axis=-1, eps=1e-5):
    m = x.mean(dim=axis, keepdim=True)
    v = x.var(dim=axis, keepdim=True, unbiased=False)
    return (x - m) / torch.sqrt(v + eps)
def _scatter_add(tgt, idx, src, axis=0):
    return tgt.clone().index_add_(axis, idx, src)
def _segment_sum(src, seg, n):
    out = torch.zeros((n,) + src.shape[1:], dtype=src.dtype)
    return out.index_add_(0, seg, src)
def _pad(x, pw):
    flat = tuple(v for pair in reversed(pw) for v in pair)
    return torch.nn.functional.pad(x, flat)

kernel_name = 'recurrent_scan__gqa_block_cached__mixer_channel_mlp__decode'

def setup_inputs():

    _w = (lambda a: (lambda t: t.to(torch.bfloat16) if t.is_floating_point() else t)(torch.as_tensor(a)))
    def _A_setup():
        B, Sq, D = 1, 1, 512
        Hq, Hkv, Sk, Dh = 32, 8, 32768, 128
        assert Hq % Hkv == 0, (Hq, Hkv)
        return {
            "x": _w((lambda *s: torch.randn(*s, dtype=torch.bfloat16) * 0.1)(B, Sq, D)),
            "k_cache": _w((lambda *s: torch.randn(*s, dtype=torch.bfloat16) * 0.8)(B, Hkv, Sk, Dh)),
            "v_cache": _w((lambda *s: torch.randn(*s, dtype=torch.bfloat16) * 3.2)(B, Hkv, Sk, Dh)),
            "wq": _w((lambda *s: torch.randn(*s, dtype=torch.bfloat16) * 0.8)(D, Hq * Dh) * (D ** -0.5)),
            "wo": _w((lambda *s: torch.randn(*s, dtype=torch.bfloat16) * 3.2)(Hq * Dh, D) * ((Hq * Dh) ** -0.5)),
        }
    _A_in = _A_setup()

    def _B_setup():
        B, S, D, F = 1, 1, 512, 1792
        return {
            "x": _w((lambda *s: torch.randn(*s, dtype=torch.bfloat16) * 0.1)(B, S, D)),
            "w1": _w((lambda *s: torch.randn(*s, dtype=torch.bfloat16) * 3.2)(D, F) * (D ** -0.5)),
            "b1": _w((lambda *s: torch.randn(*s, dtype=torch.bfloat16) * 3.2)(F)),
            "w2": _w((lambda *s: torch.randn(*s, dtype=torch.bfloat16) * 3.2)(F, D) * (F ** -0.5)),
            "b2": _w((lambda *s: torch.randn(*s, dtype=torch.bfloat16) * 3.2)(D)),
        }
    _B_in = _B_setup()

    return {
        "A_x": _A_in["x"],
        "A_k_cache": _A_in["k_cache"],
        "A_v_cache": _A_in["v_cache"],
        "A_wq": _A_in["wq"],
        "A_wo": _A_in["wo"],
        "B_x": _B_in["x"],
        "B_w1": _B_in["w1"],
        "B_b1": _B_in["b1"],
        "B_w2": _B_in["w2"],
        "B_b2": _B_in["b2"],
    }

def reference(A_x, A_k_cache, A_v_cache, A_wq, A_wo, B_x, B_w1, B_b1, B_w2, B_b2):
    def _A_ref(x, k_cache, v_cache, wq, wo):
        B, Sq, D = x.shape
        Hq, Dh = 32, 128
        Hkv = k_cache.shape[1]
        rep = Hq // Hkv
        q = (lambda x, *d: x.permute(*d))((x @ wq).reshape(B, Sq, Hq, Dh), 0, 2, 1, 3)
        k = (lambda x, n, axis: torch.repeat_interleave(x, n, dim=axis))(k_cache, rep, axis=1)
        v = (lambda x, n, axis: torch.repeat_interleave(x, n, dim=axis))(v_cache, rep, axis=1)
        scores = torch.einsum("bhqd,bhkd->bhqk", q, k) * (Dh ** -0.5)
        p = _softmax(scores, axis=-1)
        o = torch.einsum("bhqk,bhkd->bhqd", p, v)
        return (lambda x, *d: x.permute(*d))(o, 0, 2, 1, 3).reshape(B, Sq, Hq * Dh) @ wo

    def _B_ref(x, w1, b1, w2, b2):
        y = _layernorm(x, axis=-1)
        h = _gelu(torch.einsum("bsd,df->bsf", y, w1) + b1)
        return torch.einsum("bsf,fd->bsd", h, w2) + b2

    x = A_x
    xn = _layernorm(x, axis=-1)
    s = _A_ref(xn, A_k_cache, A_v_cache, A_wq, A_wo)
    g = _silu(_B_ref(xn, B_w1, B_b1, B_w2, B_b2))
    return x + s * g
```

```cpp
#include <hip/hip_runtime.h>
#include <hip/hip_bf16.h>
#include <stdint.h>

typedef unsigned v4u __attribute__((ext_vector_type(4)));
typedef float f32x4 __attribute__((ext_vector_type(4)));
typedef __attribute__((ext_vector_type(2))) __bf16 bf16x2;
typedef unsigned long long u64;
#define GAS __attribute__((address_space(1)))
typedef GAS u64 gu64;
#define RLX_AGENT __ATOMIC_RELAXED, __HIP_MEMORY_SCOPE_AGENT

constexpr int DM = 512, HQ = 32, HKV = 8, SK = 32768, DH = 128, FF = 1792;
constexpr int NWG = 256, NTHR = 512, NWAVE = 8;
constexpr int NSPLIT = 32;
constexpr int KEYS_WG = SK / NSPLIT;
constexpr int KEYS_WAVE = KEYS_WG / NWAVE;
constexpr int NLD = KEYS_WAVE / 4;
constexpr int NCH = KEYS_WAVE / 16;
constexpr int NFIN = 32;
constexpr int FCOL = DM / NFIN;
constexpr int NMLP = FF / 8;

constexpr size_t WS_E = 0;
constexpr size_t WS_QG = 64;
constexpr size_t WS_PART = WS_QG + HQ * DH;
constexpr size_t WS_PML = WS_PART + (size_t)HKV * 4 * 8 * NSPLIT * 16;
constexpr size_t WS_SP = WS_PML + (size_t)HKV * 4 * 8 * NSPLIT * 2;
constexpr size_t WS_MP = WS_SP + (size_t)NFIN * NWG * FCOL;
constexpr size_t WS_END = WS_MP + (size_t)NFIN * NWG * FCOL;

constexpr unsigned SPIN_CAP = 1u << 18;
constexpr u64 TAG_MASK = (1ull << 40) - 1ull;

__device__ __forceinline__ float bf2f(unsigned short u) { return __uint_as_float(((unsigned)u) << 16); }
__device__ __forceinline__ unsigned f2bf_bits(float f) {
  unsigned u = __float_as_uint(f);
  return (u + 0x7FFFu + ((u >> 16) & 1u)) >> 16;
}
__device__ __forceinline__ float rbf(float f) { return __uint_as_float(f2bf_bits(f) << 16); }
__device__ __forceinline__ float lo_f(unsigned w) { return __uint_as_float(w << 16); }
__device__ __forceinline__ float hi_f(unsigned w) { return __uint_as_float(w & 0xffff0000u); }

__device__ __forceinline__ u64 genc(float v, u64 tag) {
  unsigned u = (__float_as_uint(v) + 0x80u) >> 8;
  return (tag << 24) | (u64)u;
}
__device__ __forceinline__ float gdec(u64 g) { return __uint_as_float(((unsigned)g) << 8); }
__device__ __forceinline__ bool gok(u64 g, u64 tag) { return (g >> 24) == tag; }
__device__ __forceinline__ void gstore(u64* p, u64 v) { __hip_atomic_store((gu64*)p, v, RLX_AGENT); }
__device__ __forceinline__ u64 gload(const u64* p) { return __hip_atomic_load((gu64*)p, RLX_AGENT); }
__device__ __forceinline__ float up24(float v) {
  unsigned u = __float_as_uint(v);
  u = (u & 0x80000000u) ? (u & 0xffffff00u) : ((u + 0xffu) & 0xffffff00u);
  return __uint_as_float(u);
}

__device__ __forceinline__ float dot2(unsigned k, unsigned q, float acc) {
  return __builtin_amdgcn_fdot2_f32_bf16(__builtin_bit_cast(bf16x2, k), __builtin_bit_cast(bf16x2, q), acc, false);
}
template <int CTRL> __device__ __forceinline__ float dpp(float x) {
  return __builtin_bit_cast(float, __builtin_amdgcn_mov_dpp(__builtin_bit_cast(int, x), CTRL, 0xf, 0xf, true));
}
constexpr int XOR1 = 0xB1, XOR2 = 0x4E, XOR7 = 0x141, XOR8 = 0x128, ROR4 = 0x124;
__device__ __forceinline__ float xrow_max(float x) {
  auto s = __builtin_amdgcn_permlane16_swap(__float_as_uint(x), __float_as_uint(x), false, false);
  x = fmaxf(__uint_as_float(s[0]), __uint_as_float(s[1]));
  auto t = __builtin_amdgcn_permlane32_swap(__float_as_uint(x), __float_as_uint(x), false, false);
  return fmaxf(__uint_as_float(t[0]), __uint_as_float(t[1]));
}
__device__ __forceinline__ float xrow_sum(float x) {
  auto s = __builtin_amdgcn_permlane16_swap(__float_as_uint(x), __float_as_uint(x), false, false);
  x = __uint_as_float(s[0]) + __uint_as_float(s[1]);
  auto t = __builtin_amdgcn_permlane32_swap(__float_as_uint(x), __float_as_uint(x), false, false);
  return __uint_as_float(t[0]) + __uint_as_float(t[1]);
}
__device__ __forceinline__ float wave_sum(float x) {
  x += dpp<XOR1>(x); x += dpp<XOR2>(x); x += dpp<ROR4>(x); x += dpp<XOR8>(x);
  return xrow_sum(x);
}
__device__ __forceinline__ void lds_fence_barrier() {
  asm volatile("s_waitcnt lgkmcnt(0)" ::: "memory");
  __builtin_amdgcn_s_barrier();
  asm volatile("" ::: "memory");
}

__device__ __forceinline__ void ln8(const float (&v)[8], float (&o)[8]) {
  float s = 0.f;
#pragma unroll
  for (int i = 0; i < 8; ++i) s += v[i];
  s = wave_sum(s);
  const float mean = s * (1.0f / DM);
  float q = 0.f;
#pragma unroll
  for (int i = 0; i < 8; ++i) { const float d = v[i] - mean; q = fmaf(d, d, q); }
  q = wave_sum(q);
  const float var = rbf(q * (1.0f / DM));
  const float mb = rbf(mean);
  const float den = rbf(sqrtf(rbf(var + 1e-5f)));
#pragma unroll
  for (int i = 0; i < 8; ++i) o[i] = rbf(rbf(v[i] - mb) / den);
}

__device__ __forceinline__ float gelu_tanh(float x) {
  const float k0 = 0.7978845608028654f, k1 = 0.044715f;
  const float inner = k0 * (x + k1 * x * x * x);
  return 0.5f * x * (1.0f + tanhf(inner));
}

constexpr int L_Q = 0;
constexpr int L_XN = 1024;
constexpr int L_Y = 3072;
constexpr int L_P = 5120;
constexpr int L_MRG = L_P + NWAVE * 512 * 4;
constexpr int MRG_REC = 132;
constexpr int L_RED = L_MRG + NWAVE * 4 * MRG_REC * 4;
constexpr int L_WL = L_RED + 2 * NWAVE * 32 * 4;
constexpr int L_O = L_WL + 32 * 4;
constexpr int L_END = L_O + 16 * 4;

__global__ void __launch_bounds__(NTHR, 2)
fused_decode_kernel(const unsigned short* __restrict__ x, const unsigned short* __restrict__ kc,
                    const unsigned short* __restrict__ vc, const unsigned short* __restrict__ wq,
                    const unsigned short* __restrict__ wo, const unsigned short* __restrict__ w1,
                    const unsigned short* __restrict__ b1, const unsigned short* __restrict__ w2,
                    const unsigned short* __restrict__ b2, unsigned short* __restrict__ out,
                    u64* __restrict__ ws) {
  __shared__ __attribute__((aligned(16))) unsigned char lds[L_END];
  const int b = blockIdx.x;
  const int kvh = b & 7, sp = b >> 3;
  const int tid = threadIdx.x, lane = tid & 63;
  const int wave = __builtin_amdgcn_readfirstlane(tid >> 6);
  const int r = lane >> 4, c = lane & 15;

  const size_t kv_off = ((size_t)kvh * SK + (size_t)sp * KEYS_WG + (size_t)wave * KEYS_WAVE) * DH + (size_t)lane * 8;
  const v4u* kp = reinterpret_cast<const v4u*>(kc + kv_off);
  const v4u* vp = reinterpret_cast<const v4u*>(vc + kv_off);
  v4u R[NLD];
#define ISSUE_K() do { _Pragma("unroll") for (int ch_ = 0; ch_ < NCH; ++ch_) { \
    _Pragma("unroll") for (int t_ = 0; t_ < 4; ++t_) R[4 * ch_ + t_] = __builtin_nontemporal_load(kp + (4 * ch_ + t_) * 64); \
    __builtin_amdgcn_sched_barrier(0); } } while (0)

  if (wave == 0) {
    const v4u xv = *reinterpret_cast<const v4u*>(x + lane * 8);
    const int qc0 = kvh * 512 + sp * 16;
    v4u W[16];
#pragma unroll
    for (int i = 0; i < 16; ++i)
      W[i] = *reinterpret_cast<const v4u*>(wq + (size_t)((lane >> 1) + 32 * i) * (HQ * DH) + qc0 + 8 * (lane & 1));
    const u64 ep0 = gload(ws + WS_E);
    __builtin_amdgcn_sched_barrier(0);
    float xf[8], xn[8];
#pragma unroll
    for (int k = 0; k < 4; ++k) { xf[2 * k] = lo_f(xv[k]); xf[2 * k + 1] = hi_f(xv[k]); }
    ln8(xf, xn);
    float* lxn = reinterpret_cast<float*>(lds + L_XN);
    *reinterpret_cast<f32x4*>(lxn + lane * 8) = f32x4{xn[0], xn[1], xn[2], xn[3]};
    *reinterpret_cast<f32x4*>(lxn + lane * 8 + 4) = f32x4{xn[4], xn[5], xn[6], xn[7]};
    asm volatile("s_waitcnt lgkmcnt(0)" ::: "memory");
    float a[8];
#pragma unroll
    for (int k = 0; k < 8; ++k) a[k] = 0.f;
#pragma unroll
    for (int i = 0; i < 16; ++i) {
      const float xr = lxn[(lane >> 1) + 32 * i];
#pragma unroll
      for (int k = 0; k < 4; ++k) { a[2 * k] = fmaf(xr, lo_f(W[i][k]), a[2 * k]); a[2 * k + 1] = fmaf(xr, hi_f(W[i][k]), a[2 * k + 1]); }
    }
#pragma unroll
    for (int k = 0; k < 8; ++k) {
      float t = a[k];
      t += dpp<XOR2>(t); t += dpp<ROR4>(t); t += dpp<XOR8>(t);
      a[k] = xrow_sum(t);
    }
    const u64 tag = (ep0 + 1ull) & TAG_MASK;
    float qv = a[0];
#pragma unroll
    for (int k = 1; k < 8; ++k) qv = ((lane >> 1) == k) ? a[k] : qv;
    if (lane < 16) gstore(ws + WS_QG + qc0 + 8 * (lane & 1) + (lane >> 1), genc(rbf(qv), tag));
    unsigned short* lq = reinterpret_cast<unsigned short*>(lds + L_Q);
    {
      const u64* qg = ws + WS_QG + kvh * 512;
      u64 g[8];
      for (unsigned spins = 0;; ++spins) {
        bool ok = true;
#pragma unroll
        for (int i = 0; i < 8; ++i) { g[i] = gload(qg + lane + 64 * i); ok &= gok(g[i], tag); }
        if (__all(ok) || spins > SPIN_CAP) break;
        __builtin_amdgcn_s_sleep(1);
      }
#pragma unroll
      for (int i = 0; i < 8; ++i) lq[lane + 64 * i] = (unsigned short)(((unsigned)g[i] >> 8) & 0xffffu);
    }
    ISSUE_K();
    lds_fence_barrier();
  } else if (wave == 1) {
    const bool mlp = b < NMLP;
    const int bb = mlp ? b : 0;
    const v4u xv = *reinterpret_cast<const v4u*>(x + lane * 8);
    v4u W1[8], W2[8];
#pragma unroll
    for (int i = 0; i < 8; ++i) W1[i] = *reinterpret_cast<const v4u*>(w1 + (size_t)(lane + 64 * i) * FF + 8 * bb);
#pragma unroll
    for (int i = 0; i < 8; ++i) W2[i] = *reinterpret_cast<const v4u*>(w2 + (size_t)(8 * bb + i) * DM + 8 * lane);
    const v4u b1v = *reinterpret_cast<const v4u*>(b1 + 8 * bb);
    const u64 ep1 = gload(ws + WS_E);
    __builtin_amdgcn_sched_barrier(0);
    ISSUE_K();
    lds_fence_barrier();
    float xf[8], xn[8], y[8];
#pragma unroll
    for (int k = 0; k < 4; ++k) { xf[2 * k] = lo_f(xv[k]); xf[2 * k + 1] = hi_f(xv[k]); }
    ln8(xf, xn);
    ln8(xn, y);
    float* ly = reinterpret_cast<float*>(lds + L_Y);
    *reinterpret_cast<f32x4*>(ly + lane * 8) = f32x4{y[0], y[1], y[2], y[3]};
    *reinterpret_cast<f32x4*>(ly + lane * 8 + 4) = f32x4{y[4], y[5], y[6], y[7]};
    asm volatile("s_waitcnt lgkmcnt(0)" ::: "memory");
    float a[8];
#pragma unroll
    for (int k = 0; k < 8; ++k) a[k] = 0.f;
#pragma unroll
    for (int i = 0; i < 8; ++i) {
      const float yr = ly[lane + 64 * i];
#pragma unroll
      for (int k = 0; k < 4; ++k) { a[2 * k] = fmaf(yr, lo_f(W1[i][k]), a[2 * k]); a[2 * k + 1] = fmaf(yr, hi_f(W1[i][k]), a[2 * k + 1]); }
    }
    float h[8];
#pragma unroll
    for (int k = 0; k < 8; ++k) {
      const float bias = (k & 1) ? hi_f(b1v[k >> 1]) : lo_f(b1v[k >> 1]);
      h[k] = rbf(gelu_tanh(rbf(rbf(wave_sum(a[k])) + bias)));
    }
    float o[8];
#pragma unroll
    for (int k = 0; k < 8; ++k) o[k] = 0.f;
#pragma unroll
    for (int i = 0; i < 8; ++i)
#pragma unroll
      for (int k = 0; k < 4; ++k) { o[2 * k] = fmaf(h[i], lo_f(W2[i][k]), o[2 * k]); o[2 * k + 1] = fmaf(h[i], hi_f(W2[i][k]), o[2 * k + 1]); }
    if (mlp) {
      const u64 tag = (ep1 + 1ull) & TAG_MASK;
      u64* mp = ws + WS_MP + ((size_t)(lane >> 1) * NWG + b) * FCOL + (lane & 1) * 8;
#pragma unroll
      for (int k = 0; k < 8; ++k) gstore(mp + k, genc(o[k], tag));
    }
  } else {
    ISSUE_K();
    lds_fence_barrier();
  }

  v4u qpk[4];
#pragma unroll
  for (int g = 0; g < 4; ++g) qpk[g] = *reinterpret_cast<const v4u*>(lds + L_Q + (g * 128 + 8 * c) * 2);

  constexpr float C2 = 0.08838834764831845f * 1.4426950408889634f;
  float sc[NCH];
#pragma unroll
  for (int ch = 0; ch < NCH; ++ch) {
    float xs[16];
#pragma unroll
    for (int t = 0; t < 4; ++t)
#pragma unroll
      for (int g = 0; g < 4; ++g) {
        float d = 0.f;
#pragma unroll
        for (int k = 0; k < 4; ++k) d = dot2(R[4 * ch + t][k], qpk[g][k], d);
        xs[t * 4 + g] = d;
      }
#pragma unroll
    for (int t = 0; t < 4; ++t) R[4 * ch + t] = __builtin_nontemporal_load(vp + (4 * ch + t) * 64);
    __builtin_amdgcn_sched_barrier(0);
#define TR_STEP(HALF, CTRL, BIT) _Pragma("unroll") for (int hh = 0; hh < (HALF); ++hh) { \
      const float lo_ = xs[hh], hi_ = xs[hh + (HALF)]; const float t1_ = lo_ + dpp<CTRL>(lo_), t2_ = hi_ + dpp<CTRL>(hi_); \
      xs[hh] = (c & (BIT)) ? t2_ : t1_; }
    TR_STEP(8, XOR8, 8) TR_STEP(4, XOR7, 4) TR_STEP(2, XOR2, 2) TR_STEP(1, XOR1, 1)
#undef TR_STEP
    sc[ch] = xs[0] * C2;
  }
  unsigned short wov[16];
#pragma unroll
  for (int i = 0; i < 16; ++i) wov[i] = wo[(size_t)(kvh * 512 + sp * 16 + i) * DM + tid];

  const u64 epoch = gload(ws + WS_E);
  __builtin_amdgcn_sched_barrier(0);

  float mx = sc[0];
#pragma unroll
  for (int ch = 1; ch < NCH; ++ch) mx = fmaxf(mx, sc[ch]);
  mx = fmaxf(mx, dpp<ROR4>(mx)); mx = fmaxf(mx, dpp<XOR8>(mx)); mx = xrow_max(mx);
  float ls = 0.f;
  float* lp = reinterpret_cast<float*>(lds + L_P) + wave * 512;
#pragma unroll
  for (int ch = 0; ch < NCH; ++ch) {
    const float p = __builtin_amdgcn_exp2f(sc[ch] - mx);
    ls += p;
    lp[ch * 64 + lane] = p;
  }
  ls += dpp<ROR4>(ls); ls += dpp<XOR8>(ls); ls = xrow_sum(ls);
  asm volatile("s_waitcnt lgkmcnt(0)" ::: "memory");

  float acc[32];
#pragma unroll
  for (int k = 0; k < 32; ++k) acc[k] = 0.f;
#pragma unroll
  for (int ch = 0; ch < NCH; ++ch) {
    f32x4 pr[4];
#pragma unroll
    for (int t = 0; t < 4; ++t) pr[t] = *reinterpret_cast<const f32x4*>(lp + (ch * 4 + r) * 16 + 4 * t);
#pragma unroll
    for (int t = 0; t < 4; ++t) {
      float vf[8];
#pragma unroll
      for (int k = 0; k < 4; ++k) { vf[2 * k] = lo_f(R[4 * ch + t][k]); vf[2 * k + 1] = hi_f(R[4 * ch + t][k]); }
#pragma unroll
      for (int g = 0; g < 4; ++g)
#pragma unroll
        for (int k = 0; k < 8; ++k) acc[g * 8 + k] = fmaf(pr[t][g], vf[k], acc[g * 8 + k]);
    }
  }
#pragma unroll
  for (int k = 0; k < 32; ++k) acc[k] = xrow_sum(acc[k]);

  const u64 tag = (epoch + 1ull) & TAG_MASK;
  float* mrg = reinterpret_cast<float*>(lds + L_MRG);
  if (r == 0) {
#pragma unroll
    for (int g = 0; g < 4; ++g) {
      float* rec = mrg + (wave * 4 + g) * MRG_REC;
      *reinterpret_cast<f32x4*>(rec + 4 + 8 * c) = f32x4{acc[g * 8], acc[g * 8 + 1], acc[g * 8 + 2], acc[g * 8 + 3]};
      *reinterpret_cast<f32x4*>(rec + 8 + 8 * c) = f32x4{acc[g * 8 + 4], acc[g * 8 + 5], acc[g * 8 + 6], acc[g * 8 + 7]};
      if (c == g) { rec[0] = mx; rec[1] = ls; }
    }
  }
  lds_fence_barrier();
  {
    const int g = tid >> 7, d = tid & 127;
    float M = -INFINITY;
#pragma unroll
    for (int w = 0; w < NWAVE; ++w) M = fmaxf(M, mrg[(w * 4 + g) * MRG_REC]);
    M = up24(M);
    float L = 0.f, o = 0.f;
#pragma unroll
    for (int w = 0; w < NWAVE; ++w) {
      const float* rec = mrg + (w * 4 + g) * MRG_REC;
      const float wt = __builtin_amdgcn_exp2f(rec[0] - M);
      L = fmaf(wt, rec[1], L);
      o = fmaf(wt, rec[4 + d], o);
    }
    const size_t blk = (size_t)((kvh * 4 + g) * 8 + (d >> 4));
    gstore(ws + WS_PART + (blk * NSPLIT + sp) * 16 + (d & 15), genc(o, tag));
    if ((d & 15) == 0) {
      gstore(ws + WS_PML + (blk * NSPLIT + sp) * 2, genc(M, tag));
      gstore(ws + WS_PML + (blk * NSPLIT + sp) * 2 + 1, genc(L, tag));
    }
  }

  float* red = reinterpret_cast<float*>(lds + L_RED);
  float* lwl = reinterpret_cast<float*>(lds + L_WL);
  float* lo = reinterpret_cast<float*>(lds + L_O);
  {
    const size_t blk = (size_t)((kvh * 4 + (sp >> 3)) * 8 + (sp & 7));
    const u64* pa = ws + WS_PART + blk * NSPLIT * 16 + tid;
    u64 ga;
    for (unsigned spins = 0;; ++spins) {
      ga = gload(pa);
      if (__all(gok(ga, tag)) || spins > SPIN_CAP) break;
      __builtin_amdgcn_s_sleep(1);
    }
    if (wave == 0) {
      const u64* pm = ws + WS_PML + blk * NSPLIT * 2 + lane;
      u64 gm;
      for (unsigned spins = 0;; ++spins) {
        gm = gload(pm);
        if (__all(gok(gm, tag)) || spins > SPIN_CAP) break;
        __builtin_amdgcn_s_sleep(1);
      }
      const float v = gdec(gm);
      const float other = dpp<XOR1>(v);
      const float ms = (lane & 1) ? other : v, lsum = (lane & 1) ? v : other;
      float M = ms;
      M = fmaxf(M, dpp<XOR2>(M)); M = fmaxf(M, dpp<ROR4>(M)); M = fmaxf(M, dpp<XOR8>(M)); M = xrow_max(M);
      const float wt = __builtin_amdgcn_exp2f(ms - M);
      float L = (lane & 1) ? 0.f : wt * lsum;
      L = wave_sum(L);
      if (!(lane & 1)) lwl[lane >> 1] = wt / L;
    }
    lds_fence_barrier();
    float v = gdec(ga) * lwl[tid >> 4];
    v = xrow_sum(v);
    if (r == 0) red[wave * 16 + c] = v;
    lds_fence_barrier();
    if (tid < 16) {
      float o = 0.f;
#pragma unroll
      for (int w = 0; w < NWAVE; ++w) o += red[w * 16 + tid];
      lo[tid] = rbf(o);
    }
    lds_fence_barrier();
    float s = 0.f;
#pragma unroll
    for (int i = 0; i < 16; ++i) s = fmaf(lo[i], bf2f(wov[i]), s);
    gstore(ws + WS_SP + ((size_t)(tid >> 4) * NWG + b) * FCOL + (tid & 15), genc(s, tag));
  }

  if (b < NFIN) {
    const u64* spb = ws + WS_SP + (size_t)b * NWG * FCOL + tid;
    const u64* mpb = ws + WS_MP + (size_t)b * NWG * FCOL + tid;
    u64 gm[7], gs[8];
    for (unsigned spins = 0;; ++spins) {
      bool ok = true;
#pragma unroll
      for (int i = 0; i < 7; ++i) { gm[i] = gload(mpb + 512 * i); ok &= gok(gm[i], tag); }
      if (__all(ok) || spins > SPIN_CAP) break;
      __builtin_amdgcn_s_sleep(1);
    }
    for (unsigned spins = 0;; ++spins) {
      bool ok = true;
#pragma unroll
      for (int i = 0; i < 8; ++i) { gs[i] = gload(spb + 512 * i); ok &= gok(gs[i], tag); }
      if (__all(ok) || spins > SPIN_CAP) break;
      __builtin_amdgcn_s_sleep(1);
    }
    float sm = 0.f, ss = 0.f;
#pragma unroll
    for (int i = 0; i < 7; ++i) sm += gdec(gm[i]);
#pragma unroll
    for (int i = 0; i < 8; ++i) ss += gdec(gs[i]);
    sm = xrow_sum(sm); ss = xrow_sum(ss);
    float* red2 = red + NWAVE * 32;
    if (r == 0) { red2[wave * 32 + c] = sm; red2[wave * 32 + 16 + c] = ss; }
    lds_fence_barrier();
    if (tid < 16) {
      float tm = 0.f, ts = 0.f;
#pragma unroll
      for (int w = 0; w < NWAVE; ++w) { tm += red2[w * 32 + tid]; ts += red2[w * 32 + 16 + tid]; }
      const int col = b * FCOL + tid;
      const float ml = rbf(rbf(tm) + bf2f(b2[col]));
      const float gt = rbf(ml / (1.0f + __expf(-ml)));
      const float sv = rbf(ts);
      const float res = bf2f(x[col]) + rbf(sv * gt);
      out[col] = (unsigned short)f2bf_bits(res);
    }
    if (b == 0 && tid == 0) gstore(ws + WS_E, epoch + 1ull);
  }
}

extern "C" void kernel_launch(void* const* d_in, const int* in_sizes, int n_in,
                              void* d_out, int out_size, void* d_ws, size_t ws_size,
                              hipStream_t stream) {
  const unsigned short* x = (const unsigned short*)d_in[0];
  const unsigned short* kc = (const unsigned short*)d_in[1];
  const unsigned short* vc = (const unsigned short*)d_in[2];
  const unsigned short* wq = (const unsigned short*)d_in[3];
  const unsigned short* wo = (const unsigned short*)d_in[4];
  const unsigned short* w1 = (const unsigned short*)d_in[6];
  const unsigned short* b1 = (const unsigned short*)d_in[7];
  const unsigned short* w2 = (const unsigned short*)d_in[8];
  const unsigned short* b2 = (const unsigned short*)d_in[9];
  fused_decode_kernel<<<dim3(NWG), dim3(NTHR), 0, stream>>>(x, kc, vc, wq, wo, w1, b1, w2, b2,
                                                            (unsigned short*)d_out, (u64*)d_ws);
}
```

```cpp
#include <hip/hip_runtime.h>
#include <hip/hip_bf16.h>
#include <stdint.h>

typedef unsigned v4u __attribute__((ext_vector_type(4)));
typedef float f32x4 __attribute__((ext_vector_type(4)));
typedef __attribute__((ext_vector_type(2))) __bf16 bf16x2;
typedef unsigned long long u64;
#define GAS __attribute__((address_space(1)))
typedef GAS u64 gu64;
#define RLX_AGENT __ATOMIC_RELAXED, __HIP_MEMORY_SCOPE_AGENT

constexpr int DM = 512, HQ = 32, HKV = 8, SK = 32768, DH = 128, FF = 1792;
constexpr int NWG = 256, NTHR = 512, NWAVE = 8;
constexpr int NSPLIT = 32;
constexpr int KEYS_WG = SK / NSPLIT;
constexpr int NCH = 9;
constexpr int NLD = NCH * 4;
constexpr int NFIN = 32;
constexpr int FCOL = DM / NFIN;
constexpr int NMLP = FF / 8;

constexpr size_t WS_E = 0;
constexpr size_t WS_QG = 64;
constexpr size_t WS_PART = WS_QG + HQ * DH;
constexpr size_t WS_PML = WS_PART + (size_t)HKV * 4 * 8 * NSPLIT * 16;
constexpr size_t WS_SP = WS_PML + (size_t)HKV * 4 * 8 * NSPLIT * 2;
constexpr size_t WS_MP = WS_SP + (size_t)NFIN * NWG * FCOL;
constexpr size_t WS_MH = WS_MP + (size_t)NFIN * NWG * FCOL;
constexpr size_t WS_END = WS_MH + (size_t)28 * 8 * 64;

#define KWIN 16
constexpr int WIN = KWIN;
constexpr int WCH = WIN / 4;
constexpr unsigned SPIN_CAP = 1u << 18;
constexpr u64 TAG_MASK = (1ull << 40) - 1ull;

#define STAMP(i) do { } while (0)
__device__ __forceinline__ float bf2f(unsigned short u) { return __uint_as_float(((unsigned)u) << 16); }
__device__ __forceinline__ unsigned f2bf_bits(float f) {
  unsigned u = __float_as_uint(f);
  return (u + 0x7FFFu + ((u >> 16) & 1u)) >> 16;
}
__device__ __forceinline__ float rbf(float f) { return __uint_as_float(f2bf_bits(f) << 16); }
__device__ __forceinline__ float lo_f(unsigned w) { return __uint_as_float(w << 16); }
__device__ __forceinline__ float hi_f(unsigned w) { return __uint_as_float(w & 0xffff0000u); }
__device__ __forceinline__ float rfl(float v) { return __uint_as_float(__builtin_amdgcn_readfirstlane(__float_as_uint(v))); }
__device__ __forceinline__ float rdl(float v, int l) { return __uint_as_float(__builtin_amdgcn_readlane(__float_as_uint(v), l)); }
template <typename T> __device__ __forceinline__ T ntl(const T* p) { return __builtin_nontemporal_load(p); }

__device__ __forceinline__ u64 genc(float v, u64 tag) {
  unsigned u = (__float_as_uint(v) + 0x80u) >> 8;
  return (tag << 24) | (u64)u;
}
__device__ __forceinline__ float gdec(u64 g) { return __uint_as_float(((unsigned)g) << 8); }
__device__ __forceinline__ bool gok(u64 g, u64 tag) { return (g >> 24) == tag; }
__device__ __forceinline__ void gstore(u64* p, u64 v) { __hip_atomic_store((gu64*)p, v, RLX_AGENT); }
__device__ __forceinline__ u64 gload(const u64* p) { return __hip_atomic_load((gu64*)p, RLX_AGENT); }
__device__ __forceinline__ float up24(float v) {
  unsigned u = __float_as_uint(v);
  u = (u & 0x80000000u) ? (u & 0xffffff00u) : ((u + 0xffu) & 0xffffff00u);
  return __uint_as_float(u);
}

__device__ __forceinline__ float dot2(unsigned k, unsigned q, float acc) {
  return __builtin_amdgcn_fdot2_f32_bf16(__builtin_bit_cast(bf16x2, k), __builtin_bit_cast(bf16x2, q), acc, false);
}
template <int CTRL> __device__ __forceinline__ float dpp(float x) {
  return __builtin_bit_cast(float, __builtin_amdgcn_mov_dpp(__builtin_bit_cast(int, x), CTRL, 0xf, 0xf, true));
}
constexpr int XOR1 = 0xB1, XOR2 = 0x4E, XOR7 = 0x141, XOR8 = 0x128, ROR4 = 0x124;
__device__ __forceinline__ float xrow_max(float x) {
  auto s = __builtin_amdgcn_permlane16_swap(__float_as_uint(x), __float_as_uint(x), false, false);
  x = fmaxf(__uint_as_float(s[0]), __uint_as_float(s[1]));
  auto t = __builtin_amdgcn_permlane32_swap(__float_as_uint(x), __float_as_uint(x), false, false);
  return fmaxf(__uint_as_float(t[0]), __uint_as_float(t[1]));
}
__device__ __forceinline__ float xrow_sum(float x) {
  auto s = __builtin_amdgcn_permlane16_swap(__float_as_uint(x), __float_as_uint(x), false, false);
  x = __uint_as_float(s[0]) + __uint_as_float(s[1]);
  auto t = __builtin_amdgcn_permlane32_swap(__float_as_uint(x), __float_as_uint(x), false, false);
  return __uint_as_float(t[0]) + __uint_as_float(t[1]);
}
__device__ __forceinline__ float wave_sum(float x) {
  x += dpp<XOR1>(x); x += dpp<XOR2>(x); x += dpp<ROR4>(x); x += dpp<XOR8>(x);
  return xrow_sum(x);
}
__device__ __forceinline__ void lds_barrier() {
  asm volatile("s_waitcnt lgkmcnt(0)" ::: "memory");
  __builtin_amdgcn_s_barrier();
  asm volatile("" ::: "memory");
}

__device__ __forceinline__ void ln_stats(const float (&v)[8], float& mb, float& den) {
  const float piv = rfl(v[0]);
  float s1 = 0.f, s2 = 0.f;
#pragma unroll
  for (int i = 0; i < 8; ++i) { const float d = v[i] - piv; s1 += d; s2 = fmaf(d, d, s2); }
  s1 = wave_sum(s1); s2 = wave_sum(s2);
  const float md = s1 * (1.0f / DM);
  const float var = fmaxf(fmaf(-md, md, s2 * (1.0f / DM)), 0.f);
  mb = rbf(piv + md);
  den = rbf(sqrtf(rbf(rbf(var) + 1e-5f)));
}
__device__ __forceinline__ float ln_apply(float v, float mb, float den) { return rbf(rbf(v - mb) / den); }

__device__ __forceinline__ float gelu_tanh(float x) {
  const float k0 = 0.7978845608028654f, k1 = 0.044715f;
  const float inner = k0 * (x + k1 * x * x * x);
  return 0.5f * x * (1.0f + tanhf(inner));
}

#define TILE_OF(ch_) ((7 * (ch_) + rot >= 63) ? 7 * (ch_) + rot - 63 : 7 * (ch_) + rot)
typedef __attribute__((ext_vector_type(8))) __bf16 bf16x8;
typedef float f32x2 __attribute__((ext_vector_type(2)));
template <int NC>
__device__ __forceinline__ void attend(v4u (&R)[NC * 4], const v4u* __restrict__ vp, int rot, const unsigned char* lds_q,
                                       float* lp, int lane, f32x2 (&acc)[16], float& mx_out, float& ls_out) {
  const int r = lane >> 4, c = lane & 15;
  v4u qf[4];
#pragma unroll
  for (int kk = 0; kk < 4; ++kk) {
    const v4u t = *reinterpret_cast<const v4u*>(lds_q + ((c & 3) * 128 + 32 * kk + 8 * r) * 2);
    qf[kk] = (c < 4) ? t : v4u{0u, 0u, 0u, 0u};
  }
  constexpr float C2 = 0.08838834764831845f * 1.4426950408889634f;
  f32x4 mx4 = {-INFINITY, -INFINITY, -INFINITY, -INFINITY};
#pragma unroll
  for (int ch = 0; ch < NC; ++ch) {
    f32x4 d = {0.f, 0.f, 0.f, 0.f};
#pragma unroll
    for (int kk = 0; kk < 4; ++kk)
      d = __builtin_amdgcn_mfma_f32_16x16x32_bf16(__builtin_bit_cast(bf16x8, qf[kk]), __builtin_bit_cast(bf16x8, R[4 * ch + kk]), d, 0, 0, 0);
    __builtin_amdgcn_sched_barrier(0);
    if (ch < WCH) {
      const int tl = NC == 1 ? rot : TILE_OF(ch);
      if (NC > 1) asm volatile("s_waitcnt vmcnt(%0)" :: "i"(WIN - 4) : "memory");
#pragma unroll
      for (int t = 0; t < 4; ++t) R[4 * ch + t] = ntl(vp + (tl * 4 + t) * 64);
    }
    __builtin_amdgcn_sched_barrier(0);
    d *= C2;
    if (lane < 16) *reinterpret_cast<f32x4*>(lp + (ch * 16 + lane) * 4) = d;
#pragma unroll
    for (int g = 0; g < 4; ++g) mx4[g] = fmaxf(mx4[g], d[g]);
  }
#pragma unroll
  for (int g = 0; g < 4; ++g) {
    float m = mx4[g];
    m = fmaxf(m, dpp<XOR1>(m)); m = fmaxf(m, dpp<XOR2>(m)); m = fmaxf(m, dpp<ROR4>(m)); m = fmaxf(m, dpp<XOR8>(m));
    mx4[g] = rfl(m);
  }
  const int hg = lane & 3;
  const float mx = hg == 0 ? mx4[0] : (hg == 1 ? mx4[1] : (hg == 2 ? mx4[2] : mx4[3]));
  asm volatile("s_waitcnt lgkmcnt(0)" ::: "memory");
  float ls = 0.f;
#pragma unroll
  for (int j = 0; j < NC; ++j) {
    const float p = __builtin_amdgcn_exp2f(lp[j * 64 + lane] - mx);
    ls += p;
    lp[j * 64 + lane] = p;
  }
  ls += dpp<ROR4>(ls); ls += dpp<XOR8>(ls); ls = xrow_sum(ls);
  asm volatile("s_waitcnt lgkmcnt(0)" ::: "memory");
#pragma unroll
  for (int k = 0; k < 16; ++k) acc[k] = f32x2{0.f, 0.f};
#pragma unroll
  for (int i = 0; i < NC * 4; ++i) {
    if ((i & 3) == 0 && i / 4 + WCH < NC) {
      const int cn = i / 4 + WCH, tl = TILE_OF(cn);
      asm volatile("s_waitcnt vmcnt(%0)" :: "i"(WIN - 4) : "memory");
#pragma unroll
      for (int t = 0; t < 4; ++t) R[4 * cn + t] = ntl(vp + (tl * 4 + t) * 64);
      __builtin_amdgcn_sched_barrier(0);
    }
    const f32x4 pr = *reinterpret_cast<const f32x4*>(lp + (4 * i + r) * 4);
    f32x2 vf[4];
#pragma unroll
    for (int k = 0; k < 4; ++k) vf[k] = f32x2{lo_f(R[i][k]), hi_f(R[i][k])};
#pragma unroll
    for (int g = 0; g < 4; ++g) {
      const f32x2 pp = {pr[g], pr[g]};
#pragma unroll
      for (int k = 0; k < 4; ++k) acc[g * 4 + k] = __builtin_elementwise_fma(pp, vf[k], acc[g * 4 + k]);
    }
  }
  mx_out = mx; ls_out = ls;
}

constexpr int L_Q = 0;
constexpr int L_QP = 1024;
constexpr int L_Y = 1536;
constexpr int L_P = 3584;
constexpr int L_MRG = L_P + NWAVE * NCH * 64 * 4;
constexpr int L_MST = L_MRG + NWAVE * 4 * 512 * 4;
constexpr int L_RED = L_MST + NWAVE * 4 * 2 * 4;
constexpr int L_END = L_RED + 2 * NWAVE * 32 * 4;

__global__ void __launch_bounds__(NTHR, 2)
fused_decode_kernel(const unsigned short* __restrict__ x, const unsigned short* __restrict__ kc,
                    const unsigned short* __restrict__ vc, const unsigned short* __restrict__ wq,
                    const unsigned short* __restrict__ wo, const unsigned short* __restrict__ w1,
                    const unsigned short* __restrict__ b1, const unsigned short* __restrict__ w2,
                    const unsigned short* __restrict__ b2, unsigned short* __restrict__ out,
                    u64* __restrict__ ws) {
  __shared__ __attribute__((aligned(16))) unsigned char lds[L_END];
  const int b = blockIdx.x;
  const int kvh = b & 7, sp = b >> 3;
  const int tid = threadIdx.x, lane = tid & 63;
  const int wave = __builtin_amdgcn_readfirstlane(tid >> 6);
  const int r = lane >> 4, c = lane & 15;
  const bool helper = wave == 0;

  const size_t kv_off = ((size_t)kvh * SK + (size_t)sp * KEYS_WG) * DH;
  const v4u* kp = reinterpret_cast<const v4u*>(kc + kv_off + (size_t)c * DH + r * 8);
  const v4u* vp = reinterpret_cast<const v4u*>(vc + kv_off + (size_t)lane * 8);
#define ROTC 37
  const int rot = helper ? 63 : (wave - 1 + b * ROTC) % 63;

  STAMP(0);
  const int qc0 = kvh * 512 + sp * 16;
  volatile unsigned* qflag = reinterpret_cast<volatile unsigned*>(lds + L_QP);
  if (tid == 0) *qflag = 0u;
  lds_barrier();

  f32x2 acc[16]; float mx, ls;
  float* lp = reinterpret_cast<float*>(lds + L_P) + wave * (NCH * 64);
  unsigned short wov[16];
  u64 epoch;
  if (!helper) {
    v4u R[NLD];
#define KDELAY 24
    __builtin_amdgcn_s_sleep(KDELAY);
#pragma unroll
    for (int ch = 0; ch < NCH; ++ch) {
      const int tl = TILE_OF(ch);
      if (ch >= WCH) asm volatile("s_waitcnt vmcnt(%0)" :: "i"(WIN - 4) : "memory");
#pragma unroll
      for (int kk = 0; kk < 4; ++kk) R[4 * ch + kk] = ntl(kp + tl * 256 + kk * 4);
      __builtin_amdgcn_sched_barrier(0);
    }
#pragma unroll
    for (int i = 0; i < 16; ++i) wov[i] = ntl(wo + (size_t)(qc0 + i) * DM + tid);
    epoch = gload(ws + WS_E);
    __builtin_amdgcn_sched_barrier(0);
    STAMP(9);
    while (*qflag == 0u) __builtin_amdgcn_s_sleep(1);
    asm volatile("" ::: "memory");
    STAMP(1);
    attend<NCH>(R, vp, rot, lds + L_Q, lp, lane, acc, mx, ls);
  } else {
    const v4u xq = ntl(reinterpret_cast<const v4u*>(x + lane * 8));
    v4u W[16];
#pragma unroll
    for (int i = 0; i < 16; ++i)
      W[i] = ntl(reinterpret_cast<const v4u*>(wq + (size_t)((lane >> 1) + 32 * i) * (HQ * DH) + qc0 + 8 * (lane & 1)));
    epoch = gload(ws + WS_E);
    __builtin_amdgcn_sched_barrier(0);
    {
      float xf[8], mb, den;
#pragma unroll
      for (int k = 0; k < 4; ++k) { xf[2 * k] = lo_f(xq[k]); xf[2 * k + 1] = hi_f(xq[k]); }
      ln_stats(xf, mb, den);
      float* lxn = reinterpret_cast<float*>(lds + L_Y);
      *reinterpret_cast<f32x4*>(lxn + lane * 8) = f32x4{ln_apply(xf[0], mb, den), ln_apply(xf[1], mb, den), ln_apply(xf[2], mb, den), ln_apply(xf[3], mb, den)};
      *reinterpret_cast<f32x4*>(lxn + lane * 8 + 4) = f32x4{ln_apply(xf[4], mb, den), ln_apply(xf[5], mb, den), ln_apply(xf[6], mb, den), ln_apply(xf[7], mb, den)};
      asm volatile("s_waitcnt lgkmcnt(0)" ::: "memory");
      f32x2 a2[4];
#pragma unroll
      for (int k = 0; k < 4; ++k) a2[k] = f32x2{0.f, 0.f};
#pragma unroll
      for (int i = 0; i < 16; ++i) {
        const float xr = lxn[(lane >> 1) + 32 * i];
        const f32x2 xx = {xr, xr};
#pragma unroll
        for (int k = 0; k < 4; ++k) a2[k] = __builtin_elementwise_fma(xx, f32x2{lo_f(W[i][k]), hi_f(W[i][k])}, a2[k]);
      }
      float a[8];
#pragma unroll
      for (int k = 0; k < 8; ++k) { float t = a2[k >> 1][k & 1]; t += dpp<XOR2>(t); t += dpp<ROR4>(t); t += dpp<XOR8>(t); a[k] = xrow_sum(t); }
      float qv = a[0];
#pragma unroll
      for (int k = 1; k < 8; ++k) qv = ((lane >> 1) == k) ? a[k] : qv;
      STAMP(8);
      const u64 tq = (epoch + 1ull) & TAG_MASK;
      if (lane < 16) gstore(ws + WS_QG + qc0 + 8 * (lane & 1) + (lane >> 1), genc(rbf(qv), tq));
    }
    const u64 tg = (epoch + 1ull) & TAG_MASK;
    STAMP(9);
    unsigned short* lq = reinterpret_cast<unsigned short*>(lds + L_Q);
    const u64* qg = ws + WS_QG + kvh * 512;
    for (unsigned spins = 0;; ++spins) {
      const u64 t = gload(qg + (lane & 31) * 16 + 15);
      if (__all(gok(t, tg)) || spins > SPIN_CAP) break;
      __builtin_amdgcn_s_sleep(2);
    }
    u64 g[8];
    for (unsigned spins = 0;; ++spins) {
      bool ok = true;
#pragma unroll
      for (int i = 0; i < 8; ++i) { g[i] = gload(qg + lane + 64 * i); ok &= gok(g[i], tg); }
      if (__all(ok) || spins > SPIN_CAP) break;
      __builtin_amdgcn_s_sleep(2);
    }
#pragma unroll
    for (int i = 0; i < 8; ++i) lq[lane + 64 * i] = (unsigned short)(((unsigned)g[i] >> 8) & 0xffffu);
    asm volatile("s_waitcnt lgkmcnt(0)" ::: "memory");
    if (lane == 0) *qflag = 1u;
    STAMP(10);
    v4u R[4];
#pragma unroll
    for (int i = 0; i < 4; ++i) R[i] = ntl(kp + 63 * 256 + i * 4);
    const bool mlp = b < NMLP;
    const int gi = mlp ? (b >> 3) : 0, mj = b & 7;
    const v4u xv = ntl(reinterpret_cast<const v4u*>(x + lane * 8));
    v4u W1[8], W2[8];
#pragma unroll
    for (int i = 0; i < 8; ++i) W1[i] = ntl(reinterpret_cast<const v4u*>(w1 + (size_t)(64 * mj + 8 * i + (lane >> 3)) * FF + 64 * gi + 8 * (lane & 7)));
#pragma unroll
    for (int i = 0; i < 8; ++i) W2[i] = ntl(reinterpret_cast<const v4u*>(w2 + (size_t)(64 * gi + 8 * mj + i) * DM + 8 * lane));
    const unsigned short b1v = ntl(b1 + 64 * gi + 8 * mj + (lane & 7));
#pragma unroll
    for (int i = 0; i < 16; ++i) wov[i] = ntl(wo + (size_t)(qc0 + i) * DM + tid);
    float xf[8], xn[8], y[8];
#pragma unroll
    for (int k = 0; k < 4; ++k) { xf[2 * k] = lo_f(xv[k]); xf[2 * k + 1] = hi_f(xv[k]); }
    float mb, den;
    ln_stats(xf, mb, den);
#pragma unroll
    for (int k = 0; k < 8; ++k) xn[k] = ln_apply(xf[k], mb, den);
    ln_stats(xn, mb, den);
#pragma unroll
    for (int k = 0; k < 8; ++k) y[k] = ln_apply(xn[k], mb, den);
    float* ly = reinterpret_cast<float*>(lds + L_Y);
    *reinterpret_cast<f32x4*>(ly + lane * 8) = f32x4{y[0], y[1], y[2], y[3]};
    *reinterpret_cast<f32x4*>(ly + lane * 8 + 4) = f32x4{y[4], y[5], y[6], y[7]};
    asm volatile("s_waitcnt lgkmcnt(0)" ::: "memory");
    float hs[8];
#pragma unroll
    for (int k = 0; k < 8; ++k) hs[k] = 0.f;
#pragma unroll
    for (int i = 0; i < 8; ++i) {
      const float yr = ly[64 * mj + 8 * i + (lane >> 3)];
#pragma unroll
      for (int k = 0; k < 4; ++k) { hs[2 * k] = fmaf(yr, lo_f(W1[i][k]), hs[2 * k]); hs[2 * k + 1] = fmaf(yr, hi_f(W1[i][k]), hs[2 * k + 1]); }
    }
#pragma unroll
    for (int k = 0; k < 8; ++k) { float t = hs[k]; t += dpp<XOR8>(t); hs[k] = xrow_sum(t); }
    float hv = hs[0];
#pragma unroll
    for (int k = 1; k < 8; ++k) hv = ((lane >> 3) == k) ? hs[k] : hv;
    if (mlp) {
      gstore(ws + WS_MH + (((size_t)gi * 8 + (lane & 7)) * 8 + mj) * 8 + (lane >> 3), genc(hv, tg));
      u64 gh;
      const u64* ph = ws + WS_MH + ((size_t)gi * 8 + mj) * 64 + lane;
      for (unsigned spins = 0;; ++spins) {
        gh = gload(ph);
        if (__all(gok(gh, tg)) || spins > SPIN_CAP) break;
        __builtin_amdgcn_s_sleep(4);
      }
      float pre = gdec(gh);
      pre += dpp<XOR8>(pre); pre = xrow_sum(pre);
      const float hk = rbf(gelu_tanh(rbf(rbf(pre) + bf2f(b1v))));
      float o[8];
#pragma unroll
      for (int k = 0; k < 8; ++k) o[k] = 0.f;
#pragma unroll
      for (int i = 0; i < 8; ++i) {
        const float hi = rdl(hk, i);
#pragma unroll
        for (int k = 0; k < 4; ++k) { o[2 * k] = fmaf(hi, lo_f(W2[i][k]), o[2 * k]); o[2 * k + 1] = fmaf(hi, hi_f(W2[i][k]), o[2 * k + 1]); }
      }
      u64* mp = ws + WS_MP + ((size_t)(lane >> 1) * NWG + b) * FCOL + (lane & 1) * 8;
#pragma unroll
      for (int k = 0; k < 8; ++k) gstore(mp + k, genc(o[k], tg));
    }
    STAMP(1);
    attend<1>(R, vp, 63, lds + L_Q, lp, lane, acc, mx, ls);
  }
  STAMP(3);

  const u64 tag = (epoch + 1ull) & TAG_MASK;
  float* mrg = reinterpret_cast<float*>(lds + L_MRG);
  float* mst = reinterpret_cast<float*>(lds + L_MST);
  {
    float* rec = mrg + (wave * 4 + r) * 512 + 8 * c;
#pragma unroll
    for (int g = 0; g < 4; ++g) {
      *reinterpret_cast<f32x4*>(rec + g * 128) = f32x4{acc[g * 4][0], acc[g * 4][1], acc[g * 4 + 1][0], acc[g * 4 + 1][1]};
      *reinterpret_cast<f32x4*>(rec + g * 128 + 4) = f32x4{acc[g * 4 + 2][0], acc[g * 4 + 2][1], acc[g * 4 + 3][0], acc[g * 4 + 3][1]};
    }
    if (lane < 4) { mst[(wave * 4 + lane) * 2] = mx; mst[(wave * 4 + lane) * 2 + 1] = ls; }
  }
  lds_barrier();
  {
    const int g = tid >> 7, d = tid & 127;
    float mw[NWAVE], M = -INFINITY;
#pragma unroll
    for (int w = 0; w < NWAVE; ++w) { mw[w] = mst[(w * 4 + g) * 2]; M = fmaxf(M, mw[w]); }
    M = up24(M);
    float L = 0.f, o = 0.f;
#pragma unroll
    for (int w = 0; w < NWAVE; ++w) {
      const float wt = __builtin_amdgcn_exp2f(mw[w] - M);
      const float* rec = mrg + w * 2048 + tid;
      L = fmaf(wt, mst[(w * 4 + g) * 2 + 1], L);
      o = fmaf(wt, (rec[0] + rec[512]) + (rec[1024] + rec[1536]), o);
    }
    const size_t blk = (size_t)((kvh * 4 + g) * 8 + (d >> 4));
    gstore(ws + WS_PART + (blk * NSPLIT + sp) * 16 + (d & 15), genc(o, tag));
    if ((d & 15) == 0) {
      gstore(ws + WS_PML + (blk * NSPLIT + sp) * 2, genc(M, tag));
      gstore(ws + WS_PML + (blk * NSPLIT + sp) * 2 + 1, genc(L, tag));
    }
  }
  STAMP(4);

  float* red = reinterpret_cast<float*>(lds + L_RED);
  {
    const size_t blk = (size_t)((kvh * 4 + (sp >> 3)) * 8 + (sp & 7));
    const u64* pa = ws + WS_PART + blk * NSPLIT * 16 + tid;
    const u64* pm = ws + WS_PML + blk * NSPLIT * 2 + lane;
    u64 ga, gm;
    if (wave == 0) {
      for (unsigned spins = 0;; ++spins) {
        gm = gload(pm);
        if (__all(gok(gm, tag)) || spins > SPIN_CAP) break;
        __builtin_amdgcn_s_sleep(4);
      }
    }
    lds_barrier();
    for (unsigned spins = 0;; ++spins) {
      ga = gload(pa); gm = gload(pm);
      if (__all(gok(ga, tag) && gok(gm, tag)) || spins > SPIN_CAP) break;
      __builtin_amdgcn_s_sleep(2);
    }
    STAMP(5);
    const float v = gdec(gm);
    const float other = dpp<XOR1>(v);
    const float ms = (lane & 1) ? other : v, lsum = (lane & 1) ? v : other;
    float M = ms;
    M = fmaxf(M, dpp<XOR2>(M)); M = fmaxf(M, dpp<ROR4>(M)); M = fmaxf(M, dpp<XOR8>(M)); M = xrow_max(M);
    const float wt = __builtin_amdgcn_exp2f(ms - M);
    const float L = wave_sum((lane & 1) ? 0.f : wt * lsum);
    const float wsp = __shfl(wt, 2 * (4 * wave + r));
    float o = gdec(ga) * wsp;
    o = xrow_sum(o);
    if (r == 0) red[wave * 16 + c] = o;
    lds_barrier();
    float od = 0.f;
#pragma unroll
    for (int w = 0; w < NWAVE; ++w) od += red[w * 16 + c];
    od = rbf(od / L);
    float s = 0.f;
#pragma unroll
    for (int i = 0; i < 16; ++i) s = fmaf(rdl(od, i), bf2f(wov[i]), s);
    gstore(ws + WS_SP + ((size_t)(tid >> 4) * NWG + b) * FCOL + (tid & 15), genc(s, tag));
  }
  STAMP(6);

  if (b < NFIN) {
    const u64* spb = ws + WS_SP + (size_t)b * NWG * FCOL + tid;
    const u64* mpb = ws + WS_MP + (size_t)b * NWG * FCOL + tid;
    const int col = b * FCOL + (tid & 15);
    const float b2v = bf2f(ntl(b2 + col)), xv = bf2f(ntl(x + col));
    u64 gm[7], gs[8];
    for (unsigned spins = 0;; ++spins) {
      bool ok = true;
#pragma unroll
      for (int i = 0; i < 7; ++i) { gm[i] = gload(mpb + 512 * i); ok &= gok(gm[i], tag); }
#pragma unroll
      for (int i = 0; i < 8; ++i) { gs[i] = gload(spb + 512 * i); ok &= gok(gs[i], tag); }
      if (__all(ok) || spins > SPIN_CAP) break;
      __builtin_amdgcn_s_sleep(2);
    }
    float sm = 0.f, ss = 0.f;
#pragma unroll
    for (int i = 0; i < 7; ++i) sm += gdec(gm[i]);
#pragma unroll
    for (int i = 0; i < 8; ++i) ss += gdec(gs[i]);
    sm = xrow_sum(sm); ss = xrow_sum(ss);
    float* red2 = red + NWAVE * 32;
    if (r == 0) { red2[wave * 32 + c] = sm; red2[wave * 32 + 16 + c] = ss; }
    lds_barrier();
    if (tid < 16) {
      float tm = 0.f, ts = 0.f;
#pragma unroll
      for (int w = 0; w < NWAVE; ++w) { tm += red2[w * 32 + tid]; ts += red2[w * 32 + 16 + tid]; }
      const float ml = rbf(rbf(tm) + b2v);
      const float gt = rbf(ml / (1.0f + __expf(-ml)));
      const float sv = rbf(ts);
      const float res = xv + rbf(sv * gt);
      out[col] = (unsigned short)f2bf_bits(res);
    }
    STAMP(7);
    if (b == 0 && tid == 0) gstore(ws + WS_E, epoch + 1ull);
  }
}

extern "C" void kernel_launch(void* const* d_in, const int* in_sizes, int n_in,
                              void* d_out, int out_size, void* d_ws, size_t ws_size,
                              hipStream_t stream) {
  const unsigned short* x = (const unsigned short*)d_in[0];
  const unsigned short* kc = (const unsigned short*)d_in[1];
  const unsigned short* vc = (const unsigned short*)d_in[2];
  const unsigned short* wq = (const unsigned short*)d_in[3];
  const unsigned short* wo = (const unsigned short*)d_in[4];
  const unsigned short* w1 = (const unsigned short*)d_in[6];
  const unsigned short* b1 = (const unsigned short*)d_in[7];
  const unsigned short* w2 = (const unsigned short*)d_in[8];
  const unsigned short* b2 = (const unsigned short*)d_in[9];
  fused_decode_kernel<<<dim3(NWG), dim3(NTHR), 0, stream>>>(x, kc, vc, wq, wo, w1, b1, w2, b2,
                                                            (unsigned short*)d_out, (u64*)d_ws);
}
```

```cpp
#include <hip/hip_runtime.h>
#include <hip/hip_bf16.h>
#include <stdint.h>

typedef unsigned v4u __attribute__((ext_vector_type(4)));
typedef float f32x4 __attribute__((ext_vector_type(4)));
typedef unsigned long long u64;
#define GAS __attribute__((address_space(1)))
typedef GAS u64 gu64;
#define RLX_AGENT __ATOMIC_RELAXED, __HIP_MEMORY_SCOPE_AGENT

constexpr int DM = 512, HQ = 32, HKV = 8, SK = 32768, DH = 128, FF = 1792;
constexpr int NWG = 256, NTHR = 512, NWAVE = 8;
constexpr int NSPLIT = 32;
constexpr int KEYS_WG = SK / NSPLIT;
constexpr int NCH = 9;
constexpr int NLD = NCH * 4;
constexpr int NFIN = 32;
constexpr int FCOL = DM / NFIN;
constexpr int NMLP = FF / 8;

constexpr size_t WS_E = 0;
constexpr size_t WS_QG = 64;
constexpr size_t WS_PART = WS_QG + HQ * DH;
constexpr size_t WS_PML = WS_PART + (size_t)HKV * 4 * 8 * NSPLIT * 16;
constexpr size_t WS_SP = WS_PML + (size_t)HKV * 4 * 8 * NSPLIT * 2;
constexpr size_t WS_MP = WS_SP + (size_t)NFIN * NWG * FCOL;
constexpr size_t WS_MH = WS_MP + (size_t)NFIN * NWG * FCOL;
constexpr size_t WS_END = WS_MH + (size_t)28 * 8 * 64;

constexpr int WIN = 16;
constexpr int WCH = WIN / 4;
constexpr unsigned SPIN_CAP = 1u << 18;
constexpr u64 TAG_MASK = (1ull << 40) - 1ull;

__device__ __forceinline__ float bf2f(unsigned short u) { return __uint_as_float(((unsigned)u) << 16); }
__device__ __forceinline__ unsigned f2bf_bits(float f) {
  unsigned u = __float_as_uint(f);
  return (u + 0x7FFFu + ((u >> 16) & 1u)) >> 16;
}
__device__ __forceinline__ float rbf(float f) { return __uint_as_float(f2bf_bits(f) << 16); }
__device__ __forceinline__ float lo_f(unsigned w) { return __uint_as_float(w << 16); }
__device__ __forceinline__ float hi_f(unsigned w) { return __uint_as_float(w & 0xffff0000u); }
__device__ __forceinline__ float rfl(float v) { return __uint_as_float(__builtin_amdgcn_readfirstlane(__float_as_uint(v))); }
__device__ __forceinline__ float rdl(float v, int l) { return __uint_as_float(__builtin_amdgcn_readlane(__float_as_uint(v), l)); }
template <typename T> __device__ __forceinline__ T ntl(const T* p) { return __builtin_nontemporal_load(p); }

__device__ __forceinline__ u64 genc(float v, u64 tag) {
  unsigned u = (__float_as_uint(v) + 0x80u) >> 8;
  return (tag << 24) | (u64)u;
}
__device__ __forceinline__ float gdec(u64 g) { return __uint_as_float(((unsigned)g) << 8); }
__device__ __forceinline__ bool gok(u64 g, u64 tag) { return (g >> 24) == tag; }
__device__ __forceinline__ void gstore(u64* p, u64 v) { __hip_atomic_store((gu64*)p, v, RLX_AGENT); }
__device__ __forceinline__ u64 gload(const u64* p) { return __hip_atomic_load((gu64*)p, RLX_AGENT); }
__device__ __forceinline__ float up24(float v) {
  unsigned u = __float_as_uint(v);
  u = (u & 0x80000000u) ? (u & 0xffffff00u) : ((u + 0xffu) & 0xffffff00u);
  return __uint_as_float(u);
}

template <int CTRL> __device__ __forceinline__ float dpp(float x) {
  return __builtin_bit_cast(float, __builtin_amdgcn_mov_dpp(__builtin_bit_cast(int, x), CTRL, 0xf, 0xf, true));
}
constexpr int XOR1 = 0xB1, XOR2 = 0x4E, XOR7 = 0x141, XOR8 = 0x128, ROR4 = 0x124;
__device__ __forceinline__ float xrow_max(float x) {
  auto s = __builtin_amdgcn_permlane16_swap(__float_as_uint(x), __float_as_uint(x), false, false);
  x = fmaxf(__uint_as_float(s[0]), __uint_as_float(s[1]));
  auto t = __builtin_amdgcn_permlane32_swap(__float_as_uint(x), __float_as_uint(x), false, false);
  return fmaxf(__uint_as_float(t[0]), __uint_as_float(t[1]));
}
__device__ __forceinline__ float xrow_sum(float x) {
  auto s = __builtin_amdgcn_permlane16_swap(__float_as_uint(x), __float_as_uint(x), false, false);
  x = __uint_as_float(s[0]) + __uint_as_float(s[1]);
  auto t = __builtin_amdgcn_permlane32_swap(__float_as_uint(x), __float_as_uint(x), false, false);
  return __uint_as_float(t[0]) + __uint_as_float(t[1]);
}
__device__ __forceinline__ float wave_sum(float x) {
  x += dpp<XOR1>(x); x += dpp<XOR2>(x); x += dpp<ROR4>(x); x += dpp<XOR8>(x);
  return xrow_sum(x);
}
__device__ __forceinline__ void lds_barrier() {
  asm volatile("s_waitcnt lgkmcnt(0)" ::: "memory");
  __builtin_amdgcn_s_barrier();
  asm volatile("" ::: "memory");
}

__device__ __forceinline__ void ln_stats(const float (&v)[8], float& mb, float& den) {
  const float piv = rfl(v[0]);
  float s1 = 0.f, s2 = 0.f;
#pragma unroll
  for (int i = 0; i < 8; ++i) { const float d = v[i] - piv; s1 += d; s2 = fmaf(d, d, s2); }
  s1 = wave_sum(s1); s2 = wave_sum(s2);
  const float md = s1 * (1.0f / DM);
  const float var = fmaxf(fmaf(-md, md, s2 * (1.0f / DM)), 0.f);
  mb = rbf(piv + md);
  den = rbf(sqrtf(rbf(rbf(var) + 1e-5f)));
}
__device__ __forceinline__ float ln_apply(float v, float mb, float den) { return rbf(rbf(v - mb) / den); }

__device__ __forceinline__ float gelu_tanh(float x) {
  const float k0 = 0.7978845608028654f, k1 = 0.044715f;
  const float inner = k0 * (x + k1 * x * x * x);
  return 0.5f * x * (1.0f + tanhf(inner));
}

#define TILE_OF(ch_) ((7 * (ch_) + rot >= 63) ? 7 * (ch_) + rot - 63 : 7 * (ch_) + rot)
typedef __attribute__((ext_vector_type(8))) __bf16 bf16x8;
typedef float f32x2 __attribute__((ext_vector_type(2)));
template <int NC>
__device__ __forceinline__ void attend(v4u (&R)[NC * 4], const v4u* __restrict__ vp, int rot, const unsigned char* lds_q,
                                       float* lp, unsigned char* stg, int lane, f32x2 (&acc)[16], float& mx_out, float& ls_out) {
  const int r = lane >> 4, c = lane & 15;
  v4u qf[4];
#pragma unroll
  for (int kk = 0; kk < 4; ++kk) {
    const v4u t = *reinterpret_cast<const v4u*>(lds_q + ((c & 3) * 128 + 32 * kk + 8 * r) * 2);
    qf[kk] = (c < 4) ? t : v4u{0u, 0u, 0u, 0u};
  }
  constexpr float C2 = 0.08838834764831845f * 1.4426950408889634f;
  f32x4 mx4 = {-INFINITY, -INFINITY, -INFINITY, -INFINITY};
#pragma unroll
  for (int ch = 0; ch < NC; ++ch) {
#pragma unroll
    for (int t = 0; t < 4; ++t) *reinterpret_cast<v4u*>(stg + (4 * t + r) * 272 + 16 * c) = R[4 * ch + t];
    asm volatile("s_waitcnt lgkmcnt(0)" ::: "memory");
    v4u kf[4];
#pragma unroll
    for (int kk = 0; kk < 4; ++kk) kf[kk] = *reinterpret_cast<const v4u*>(stg + c * 272 + 64 * kk + 16 * r);
    asm volatile("s_waitcnt lgkmcnt(0)" ::: "memory");
    f32x4 d = {0.f, 0.f, 0.f, 0.f};
#pragma unroll
    for (int kk = 0; kk < 4; ++kk)
      d = __builtin_amdgcn_mfma_f32_16x16x32_bf16(__builtin_bit_cast(bf16x8, qf[kk]), __builtin_bit_cast(bf16x8, kf[kk]), d, 0, 0, 0);
    __builtin_amdgcn_sched_barrier(0);
    if (ch < WCH) {
      const int tl = NC == 1 ? rot : TILE_OF(ch);
      if (NC > 1) asm volatile("s_waitcnt vmcnt(%0)" :: "i"(WIN - 4) : "memory");
#pragma unroll
      for (int t = 0; t < 4; ++t) R[4 * ch + t] = ntl(vp + (tl * 4 + t) * 64);
    }
    __builtin_amdgcn_sched_barrier(0);
    d *= C2;
    if (lane < 16) *reinterpret_cast<f32x4*>(lp + (ch * 16 + lane) * 4) = d;
#pragma unroll
    for (int g = 0; g < 4; ++g) mx4[g] = fmaxf(mx4[g], d[g]);
  }
#pragma unroll
  for (int g = 0; g < 4; ++g) {
    float m = mx4[g];
    m = fmaxf(m, dpp<XOR1>(m)); m = fmaxf(m, dpp<XOR2>(m)); m = fmaxf(m, dpp<ROR4>(m)); m = fmaxf(m, dpp<XOR8>(m));
    mx4[g] = rfl(m);
  }
  const int hg = lane & 3;
  const float mx = hg == 0 ? mx4[0] : (hg == 1 ? mx4[1] : (hg == 2 ? mx4[2] : mx4[3]));
  asm volatile("s_waitcnt lgkmcnt(0)" ::: "memory");
  float ls = 0.f;
#pragma unroll
  for (int j = 0; j < NC; ++j) {
    const float p = __builtin_amdgcn_exp2f(lp[j * 64 + lane] - mx);
    ls += p;
    lp[j * 64 + lane] = p;
  }
  ls += dpp<ROR4>(ls); ls += dpp<XOR8>(ls); ls = xrow_sum(ls);
  asm volatile("s_waitcnt lgkmcnt(0)" ::: "memory");
#pragma unroll
  for (int k = 0; k < 16; ++k) acc[k] = f32x2{0.f, 0.f};
#pragma unroll
  for (int i = 0; i < NC * 4; ++i) {
    if ((i & 3) == 0 && i / 4 + WCH < NC) {
      const int cn = i / 4 + WCH, tl = TILE_OF(cn);
      asm volatile("s_waitcnt vmcnt(%0)" :: "i"(WIN - 4) : "memory");
#pragma unroll
      for (int t = 0; t < 4; ++t) R[4 * cn + t] = ntl(vp + (tl * 4 + t) * 64);
      __builtin_amdgcn_sched_barrier(0);
    }
    const f32x4 pr = *reinterpret_cast<const f32x4*>(lp + (4 * i + r) * 4);
    f32x2 vf[4];
#pragma unroll
    for (int k = 0; k < 4; ++k) vf[k] = f32x2{lo_f(R[i][k]), hi_f(R[i][k])};
#pragma unroll
    for (int g = 0; g < 4; ++g) {
      const f32x2 pp = {pr[g], pr[g]};
#pragma unroll
      for (int k = 0; k < 4; ++k) acc[g * 4 + k] = __builtin_elementwise_fma(pp, vf[k], acc[g * 4 + k]);
    }
  }
  mx_out = mx; ls_out = ls;
}

constexpr int L_Q = 0;
constexpr int L_QP = 1024;
constexpr int L_Y = 1536;
constexpr int L_P = 3584;
constexpr int L_MRG = L_P + NWAVE * NCH * 64 * 4;
constexpr int L_MST = L_MRG + NWAVE * 4 * 512 * 4;
constexpr int L_RED = L_MST + NWAVE * 4 * 2 * 4;
constexpr int L_STG = L_RED + 2 * NWAVE * 32 * 4;
constexpr int L_END = L_STG + NWAVE * 4352;

__global__ void __launch_bounds__(NTHR, 2)
fused_decode_kernel(const unsigned short* __restrict__ x, const unsigned short* __restrict__ kc,
                    const unsigned short* __restrict__ vc, const unsigned short* __restrict__ wq,
                    const unsigned short* __restrict__ wo, const unsigned short* __restrict__ w1,
                    const unsigned short* __restrict__ b1, const unsigned short* __restrict__ w2,
                    const unsigned short* __restrict__ b2, unsigned short* __restrict__ out,
                    u64* __restrict__ ws) {
  __shared__ __attribute__((aligned(16))) unsigned char lds[L_END];
  const int b = blockIdx.x;
  const int kvh = b & 7, sp = b >> 3;
  const int tid = threadIdx.x, lane = tid & 63;
  const int wave = __builtin_amdgcn_readfirstlane(tid >> 6);
  const int r = lane >> 4, c = lane & 15;
  const bool helper = wave == 0;

  const size_t kv_off = ((size_t)kvh * SK + (size_t)sp * KEYS_WG) * DH;
  const v4u* kp = reinterpret_cast<const v4u*>(kc + kv_off + (size_t)lane * 8);
  const v4u* vp = reinterpret_cast<const v4u*>(vc + kv_off + (size_t)lane * 8);
  const int rot = helper ? 63 : (wave - 1 + b * 37) % 63;

  const int qc0 = kvh * 512 + sp * 16;
  volatile unsigned* qflag = reinterpret_cast<volatile unsigned*>(lds + L_QP);
  if (tid == 0) *qflag = 0u;
  lds_barrier();

  f32x2 acc[16]; float mx, ls;
  float* lp = reinterpret_cast<float*>(lds + L_P) + wave * (NCH * 64);
  unsigned short wov[16];
  u64 epoch;
  if (!helper) {
    v4u R[NLD];
#pragma unroll
    for (int ch = 0; ch < NCH; ++ch) {
      const int tl = TILE_OF(ch);
      if (ch >= WCH) asm volatile("s_waitcnt vmcnt(%0)" :: "i"(WIN - 4) : "memory");
#pragma unroll
      for (int kk = 0; kk < 4; ++kk) R[4 * ch + kk] = ntl(kp + tl * 256 + kk * 64);
      __builtin_amdgcn_sched_barrier(0);
    }
#pragma unroll
    for (int i = 0; i < 16; ++i) wov[i] = ntl(wo + (size_t)(qc0 + i) * DM + tid);
    epoch = gload(ws + WS_E);
    __builtin_amdgcn_sched_barrier(0);
    while (*qflag == 0u) __builtin_amdgcn_s_sleep(1);
    asm volatile("" ::: "memory");
    attend<NCH>(R, vp, rot, lds + L_Q, lp, lds + L_STG + wave * 4352, lane, acc, mx, ls);
  } else {
    const v4u xq = ntl(reinterpret_cast<const v4u*>(x + lane * 8));
    v4u W[16];
#pragma unroll
    for (int i = 0; i < 16; ++i)
      W[i] = ntl(reinterpret_cast<const v4u*>(wq + (size_t)((lane >> 1) + 32 * i) * (HQ * DH) + qc0 + 8 * (lane & 1)));
    epoch = gload(ws + WS_E);
    __builtin_amdgcn_sched_barrier(0);
    {
      float xf[8], mb, den;
#pragma unroll
      for (int k = 0; k < 4; ++k) { xf[2 * k] = lo_f(xq[k]); xf[2 * k + 1] = hi_f(xq[k]); }
      ln_stats(xf, mb, den);
      float* lxn = reinterpret_cast<float*>(lds + L_Y);
      *reinterpret_cast<f32x4*>(lxn + lane * 8) = f32x4{ln_apply(xf[0], mb, den), ln_apply(xf[1], mb, den), ln_apply(xf[2], mb, den), ln_apply(xf[3], mb, den)};
      *reinterpret_cast<f32x4*>(lxn + lane * 8 + 4) = f32x4{ln_apply(xf[4], mb, den), ln_apply(xf[5], mb, den), ln_apply(xf[6], mb, den), ln_apply(xf[7], mb, den)};
      asm volatile("s_waitcnt lgkmcnt(0)" ::: "memory");
      f32x2 a2[4];
#pragma unroll
      for (int k = 0; k < 4; ++k) a2[k] = f32x2{0.f, 0.f};
#pragma unroll
      for (int i = 0; i < 16; ++i) {
        const float xr = lxn[(lane >> 1) + 32 * i];
        const f32x2 xx = {xr, xr};
#pragma unroll
        for (int k = 0; k < 4; ++k) a2[k] = __builtin_elementwise_fma(xx, f32x2{lo_f(W[i][k]), hi_f(W[i][k])}, a2[k]);
      }
      float a[8];
#pragma unroll
      for (int k = 0; k < 8; ++k) { float t = a2[k >> 1][k & 1]; t += dpp<XOR2>(t); t += dpp<ROR4>(t); t += dpp<XOR8>(t); a[k] = xrow_sum(t); }
      float qv = a[0];
#pragma unroll
      for (int k = 1; k < 8; ++k) qv = ((lane >> 1) == k) ? a[k] : qv;
      const u64 tq = (epoch + 1ull) & TAG_MASK;
      if (lane < 16) gstore(ws + WS_QG + qc0 + 8 * (lane & 1) + (lane >> 1), genc(rbf(qv), tq));
    }
    const u64 tg = (epoch + 1ull) & TAG_MASK;
    unsigned short* lq = reinterpret_cast<unsigned short*>(lds + L_Q);
    const u64* qg = ws + WS_QG + kvh * 512;
    u64 g[8];
    for (unsigned spins = 0;; ++spins) {
      bool ok = true;
#pragma unroll
      for (int i = 0; i < 8; ++i) { g[i] = gload(qg + lane + 64 * i); ok &= gok(g[i], tg); }
      if (__all(ok) || spins > SPIN_CAP) break;
      __builtin_amdgcn_s_sleep(2);
    }
#pragma unroll
    for (int i = 0; i < 8; ++i) lq[lane + 64 * i] = (unsigned short)(((unsigned)g[i] >> 8) & 0xffffu);
    asm volatile("s_waitcnt lgkmcnt(0)" ::: "memory");
    if (lane == 0) *qflag = 1u;
    v4u R[4];
#pragma unroll
    for (int i = 0; i < 4; ++i) R[i] = ntl(kp + 63 * 256 + i * 64);
    const bool mlp = b < NMLP;
    const int gi = mlp ? (b >> 3) : 0, mj = b & 7;
    const v4u xv = ntl(reinterpret_cast<const v4u*>(x + lane * 8));
    v4u W1[8], W2[8];
#pragma unroll
    for (int i = 0; i < 8; ++i) W1[i] = ntl(reinterpret_cast<const v4u*>(w1 + (size_t)(64 * mj + 8 * i + (lane >> 3)) * FF + 64 * gi + 8 * (lane & 7)));
#pragma unroll
    for (int i = 0; i < 8; ++i) W2[i] = ntl(reinterpret_cast<const v4u*>(w2 + (size_t)(64 * gi + 8 * mj + i) * DM + 8 * lane));
    const unsigned short b1v = ntl(b1 + 64 * gi + 8 * mj + (lane & 7));
#pragma unroll
    for (int i = 0; i < 16; ++i) wov[i] = ntl(wo + (size_t)(qc0 + i) * DM + tid);
    float xf[8], xn[8], y[8];
#pragma unroll
    for (int k = 0; k < 4; ++k) { xf[2 * k] = lo_f(xv[k]); xf[2 * k + 1] = hi_f(xv[k]); }
    float mb, den;
    ln_stats(xf, mb, den);
#pragma unroll
    for (int k = 0; k < 8; ++k) xn[k] = ln_apply(xf[k], mb, den);
    ln_stats(xn, mb, den);
#pragma unroll
    for (int k = 0; k < 8; ++k) y[k] = ln_apply(xn[k], mb, den);
    float* ly = reinterpret_cast<float*>(lds + L_Y);
    *reinterpret_cast<f32x4*>(ly + lane * 8) = f32x4{y[0], y[1], y[2], y[3]};
    *reinterpret_cast<f32x4*>(ly + lane * 8 + 4) = f32x4{y[4], y[5], y[6], y[7]};
    asm volatile("s_waitcnt lgkmcnt(0)" ::: "memory");
    float hs[8];
#pragma unroll
    for (int k = 0; k < 8; ++k) hs[k] = 0.f;
#pragma unroll
    for (int i = 0; i < 8; ++i) {
      const float yr = ly[64 * mj + 8 * i + (lane >> 3)];
#pragma unroll
      for (int k = 0; k < 4; ++k) { hs[2 * k] = fmaf(yr, lo_f(W1[i][k]), hs[2 * k]); hs[2 * k + 1] = fmaf(yr, hi_f(W1[i][k]), hs[2 * k + 1]); }
    }
#pragma unroll
    for (int k = 0; k < 8; ++k) { float t = hs[k]; t += dpp<XOR8>(t); hs[k] = xrow_sum(t); }
    float hv = hs[0];
#pragma unroll
    for (int k = 1; k < 8; ++k) hv = ((lane >> 3) == k) ? hs[k] : hv;
    if (mlp) {
      gstore(ws + WS_MH + (((size_t)gi * 8 + (lane & 7)) * 8 + mj) * 8 + (lane >> 3), genc(hv, tg));
      u64 gh;
      const u64* ph = ws + WS_MH + ((size_t)gi * 8 + mj) * 64 + lane;
      for (unsigned spins = 0;; ++spins) {
        gh = gload(ph);
        if (__all(gok(gh, tg)) || spins > SPIN_CAP) break;
        __builtin_amdgcn_s_sleep(4);
      }
      float pre = gdec(gh);
      pre += dpp<XOR8>(pre); pre = xrow_sum(pre);
      const float hk = rbf(gelu_tanh(rbf(rbf(pre) + bf2f(b1v))));
      float o[8];
#pragma unroll
      for (int k = 0; k < 8; ++k) o[k] = 0.f;
#pragma unroll
      for (int i = 0; i < 8; ++i) {
        const float hi = rdl(hk, i);
#pragma unroll
        for (int k = 0; k < 4; ++k) { o[2 * k] = fmaf(hi, lo_f(W2[i][k]), o[2 * k]); o[2 * k + 1] = fmaf(hi, hi_f(W2[i][k]), o[2 * k + 1]); }
      }
      u64* mp = ws + WS_MP + ((size_t)(lane >> 1) * NWG + b) * FCOL + (lane & 1) * 8;
#pragma unroll
      for (int k = 0; k < 8; ++k) gstore(mp + k, genc(o[k], tg));
    }
    attend<1>(R, vp, 63, lds + L_Q, lp, lds + L_STG + wave * 4352, lane, acc, mx, ls);
  }

  const u64 tag = (epoch + 1ull) & TAG_MASK;
  float* mrg = reinterpret_cast<float*>(lds + L_MRG);
  float* mst = reinterpret_cast<float*>(lds + L_MST);
  {
    float* rec = mrg + (wave * 4 + r) * 512 + 8 * c;
#pragma unroll
    for (int g = 0; g < 4; ++g) {
      *reinterpret_cast<f32x4*>(rec + g * 128) = f32x4{acc[g * 4][0], acc[g * 4][1], acc[g * 4 + 1][0], acc[g * 4 + 1][1]};
      *reinterpret_cast<f32x4*>(rec + g * 128 + 4) = f32x4{acc[g * 4 + 2][0], acc[g * 4 + 2][1], acc[g * 4 + 3][0], acc[g * 4 + 3][1]};
    }
    if (lane < 4) { mst[(wave * 4 + lane) * 2] = mx; mst[(wave * 4 + lane) * 2 + 1] = ls; }
  }
  lds_barrier();
  {
    const int g = tid >> 7, d = tid & 127;
    float mw[NWAVE], M = -INFINITY;
#pragma unroll
    for (int w = 0; w < NWAVE; ++w) { mw[w] = mst[(w * 4 + g) * 2]; M = fmaxf(M, mw[w]); }
    M = up24(M);
    float L = 0.f, o = 0.f;
#pragma unroll
    for (int w = 0; w < NWAVE; ++w) {
      const float wt = __builtin_amdgcn_exp2f(mw[w] - M);
      const float* rec = mrg + w * 2048 + tid;
      L = fmaf(wt, mst[(w * 4 + g) * 2 + 1], L);
      o = fmaf(wt, (rec[0] + rec[512]) + (rec[1024] + rec[1536]), o);
    }
    const size_t blk = (size_t)((kvh * 4 + g) * 8 + (d >> 4));
    gstore(ws + WS_PART + (blk * NSPLIT + sp) * 16 + (d & 15), genc(o, tag));
    if ((d & 15) == 0) {
      gstore(ws + WS_PML + (blk * NSPLIT + sp) * 2, genc(M, tag));
      gstore(ws + WS_PML + (blk * NSPLIT + sp) * 2 + 1, genc(L, tag));
    }
  }

  float* red = reinterpret_cast<float*>(lds + L_RED);
  {
    if (wave == 0) {
      const size_t blk = (size_t)((kvh * 4 + (sp >> 3)) * 8 + (sp & 7));
      const u64* pa = ws + WS_PART + blk * NSPLIT * 16 + lane;
      const u64* pm = ws + WS_PML + blk * NSPLIT * 2 + lane;
      u64 ga[8], gm;
      for (unsigned spins = 0;; ++spins) {
        gm = gload(pm);
        bool ok = gok(gm, tag);
#pragma unroll
        for (int i = 0; i < 8; ++i) { ga[i] = gload(pa + 64 * i); ok &= gok(ga[i], tag); }
        if (__all(ok) || spins > SPIN_CAP) break;
        __builtin_amdgcn_s_sleep(4);
      }
      const float v = gdec(gm);
      const float other = dpp<XOR1>(v);
      const float ms = (lane & 1) ? other : v, lsum = (lane & 1) ? v : other;
      float M = ms;
      M = fmaxf(M, dpp<XOR2>(M)); M = fmaxf(M, dpp<ROR4>(M)); M = fmaxf(M, dpp<XOR8>(M)); M = xrow_max(M);
      const float wt = __builtin_amdgcn_exp2f(ms - M);
      const float L = wave_sum((lane & 1) ? 0.f : wt * lsum);
      if (!(lane & 1)) red[lane >> 1] = wt;
      asm volatile("s_waitcnt lgkmcnt(0)" ::: "memory");
      float o = 0.f;
#pragma unroll
      for (int i = 0; i < 8; ++i) o = fmaf(gdec(ga[i]), red[4 * i + r], o);
      o = xrow_sum(o);
      if (lane < 16) red[32 + lane] = rbf(o / L);
    }
    lds_barrier();
    const f32x4 o0 = *reinterpret_cast<const f32x4*>(red + 32), o1 = *reinterpret_cast<const f32x4*>(red + 36);
    const f32x4 o2 = *reinterpret_cast<const f32x4*>(red + 40), o3 = *reinterpret_cast<const f32x4*>(red + 44);
    float s0 = 0.f, s1 = 0.f;
#pragma unroll
    for (int i = 0; i < 4; ++i) {
      s0 = fmaf(o0[i], bf2f(wov[i]), s0); s1 = fmaf(o1[i], bf2f(wov[4 + i]), s1);
      s0 = fmaf(o2[i], bf2f(wov[8 + i]), s0); s1 = fmaf(o3[i], bf2f(wov[12 + i]), s1);
    }
    gstore(ws + WS_SP + ((size_t)(tid >> 4) * NWG + b) * FCOL + (tid & 15), genc(s0 + s1, tag));
  }

  if (b < NFIN) {
    const u64* spb = ws + WS_SP + (size_t)b * NWG * FCOL + tid;
    const u64* mpb = ws + WS_MP + (size_t)b * NWG * FCOL + tid;
    const int col = b * FCOL + (tid & 15);
    const float b2v = bf2f(ntl(b2 + col)), xv = bf2f(ntl(x + col));
    u64 gm[7], gs[8];
    for (unsigned spins = 0;; ++spins) {
      bool ok = true;
#pragma unroll
      for (int i = 0; i < 7; ++i) { gm[i] = gload(mpb + 512 * i); ok &= gok(gm[i], tag); }
#pragma unroll
      for (int i = 0; i < 8; ++i) { gs[i] = gload(spb + 512 * i); ok &= gok(gs[i], tag); }
      if (__all(ok) || spins > SPIN_CAP) break;
      __builtin_amdgcn_s_sleep(2);
    }
    float sm = 0.f, ss = 0.f;
#pragma unroll
    for (int i = 0; i < 7; ++i) sm += gdec(gm[i]);
#pragma unroll
    for (int i = 0; i < 8; ++i) ss += gdec(gs[i]);
    sm = xrow_sum(sm); ss = xrow_sum(ss);
    float* red2 = red + NWAVE * 32;
    if (r == 0) { red2[wave * 32 + c] = sm; red2[wave * 32 + 16 + c] = ss; }
    lds_barrier();
    if (tid < 16) {
      float tm = 0.f, ts = 0.f;
#pragma unroll
      for (int w = 0; w < NWAVE; ++w) { tm += red2[w * 32 + tid]; ts += red2[w * 32 + 16 + tid]; }
      const float ml = rbf(rbf(tm) + b2v);
      const float gt = rbf(ml / (1.0f + __expf(-ml)));
      const float sv = rbf(ts);
      const float res = xv + rbf(sv * gt);
      out[col] = (unsigned short)f2bf_bits(res);
    }
    if (b == 0 && tid == 0) gstore(ws + WS_E, epoch + 1ull);
  }
}

extern "C" void kernel_launch(void* const* d_in, const int* in_sizes, int n_in,
                              void* d_out, int out_size, void* d_ws, size_t ws_size,
                              hipStream_t stream) {
  const unsigned short* x = (const unsigned short*)d_in[0];
  const unsigned short* kc = (const unsigned short*)d_in[1];
  const unsigned short* vc = (const unsigned short*)d_in[2];
  const unsigned short* wq = (const unsigned short*)d_in[3];
  const unsigned short* wo = (const unsigned short*)d_in[4];
  const unsigned short* w1 = (const unsigned short*)d_in[6];
  const unsigned short* b1 = (const unsigned short*)d_in[7];
  const unsigned short* w2 = (const unsigned short*)d_in[8];
  const unsigned short* b2 = (const unsigned short*)d_in[9];
  fused_decode_kernel<<<dim3(NWG), dim3(NTHR), 0, stream>>>(x, kc, vc, wq, wo, w1, b1, w2, b2,
                                                            (unsigned short*)d_out, (u64*)d_ws);
}
```
